```python
import torch

def _softmax(x, axis=-1):
    return torch.softmax(x, dim=axis)
def _gelu(x):
    return torch.nn.functional.gelu(x, approximate="tanh")
def _silu(x):
    return torch.nn.functional.silu(x)
def _sigmoid(x):
    return torch.sigmoid(x)
def _layernorm(x, axis=-1, eps=1e-5):
    m = x.mean(dim=axis, keepdim=True)
    v = x.var(dim=axis, keepdim=True, unbiased=False)
    return (x - m) / torch.sqrt(v + eps)
def _scatter_add(tgt, idx, src, axis=0):
    return tgt.clone().index_add_(axis, idx, src)
def _segment_sum(src, seg, n):
    out = torch.zeros((n,) + src.shape[1:], dtype=src.dtype)
    return out.index_add_(0, seg, src)
def _pad(x, pw):
    flat = tuple(v for pair in reversed(pw) for v in pair)
    return torch.nn.functional.pad(x, flat)

kernel_name = 'recurrent_scan__mha_block_cached__mixer_channel_mlp__decode'

def setup_inputs():

    _w = (lambda a: (lambda t: t.to(torch.bfloat16) if t.is_floating_point() else t)(torch.as_tensor(a)))
    def _A_setup():
        B, Sq, D = 4, 1, 512
        H, Sk, Dh = 32, 4096, 128
        return {
            "x": _w((lambda *s: torch.randn(*s, dtype=torch.bfloat16) * 0.1)(B, Sq, D)),
            "k_cache": _w((lambda *s: torch.randn(*s, dtype=torch.bfloat16) * 0.8)(B, H, Sk, Dh)),
            "v_cache": _w((lambda *s: torch.randn(*s, dtype=torch.bfloat16) * 3.2)(B, H, Sk, Dh)),
            "wq": _w((lambda *s: torch.randn(*s, dtype=torch.bfloat16) * 0.8)(D, H * Dh) * (D ** -0.5)),
            "wo": _w((lambda *s: torch.randn(*s, dtype=torch.bfloat16) * 3.2)(H * Dh, D) * ((H * Dh) ** -0.5)),
        }
    _A_in = _A_setup()

    def _B_setup():
        B, S, D, F = 4, 1, 512, 1792
        return {
            "x": _w((lambda *s: torch.randn(*s, dtype=torch.bfloat16) * 0.1)(B, S, D)),
            "w1": _w((lambda *s: torch.randn(*s, dtype=torch.bfloat16) * 3.2)(D, F) * (D ** -0.5)),
            "b1": _w((lambda *s: torch.randn(*s, dtype=torch.bfloat16) * 3.2)(F)),
            "w2": _w((lambda *s: torch.randn(*s, dtype=torch.bfloat16) * 3.2)(F, D) * (F ** -0.5)),
            "b2": _w((lambda *s: torch.randn(*s, dtype=torch.bfloat16) * 3.2)(D)),
        }
    _B_in = _B_setup()

    return {
        "A_x": _A_in["x"],
        "A_k_cache": _A_in["k_cache"],
        "A_v_cache": _A_in["v_cache"],
        "A_wq": _A_in["wq"],
        "A_wo": _A_in["wo"],
        "B_x": _B_in["x"],
        "B_w1": _B_in["w1"],
        "B_b1": _B_in["b1"],
        "B_w2": _B_in["w2"],
        "B_b2": _B_in["b2"],
    }

def reference(A_x, A_k_cache, A_v_cache, A_wq, A_wo, B_x, B_w1, B_b1, B_w2, B_b2):
    def _A_ref(x, k_cache, v_cache, wq, wo):
        B, Sq, D = x.shape
        H, Dh = 32, 128
        q = (lambda x, *d: x.permute(*d))((x @ wq).reshape(B, Sq, H, Dh), 0, 2, 1, 3)
        scores = torch.einsum("bhqd,bhkd->bhqk", q, k_cache) * (Dh ** -0.5)
        p = _softmax(scores, axis=-1)
        o = torch.einsum("bhqk,bhkd->bhqd", p, v_cache)
        return (lambda x, *d: x.permute(*d))(o, 0, 2, 1, 3).reshape(B, Sq, H * Dh) @ wo

    def _B_ref(x, w1, b1, w2, b2):
        y = _layernorm(x, axis=-1)
        h = _gelu(torch.einsum("bsd,df->bsf", y, w1) + b1)
        return torch.einsum("bsf,fd->bsd", h, w2) + b2

    x = A_x
    xn = _layernorm(x, axis=-1)
    s = _A_ref(xn, A_k_cache, A_v_cache, A_wq, A_wo)
    g = _silu(_B_ref(xn, B_w1, B_b1, B_w2, B_b2))
    return x + s * g
```

```cpp
#include <hip/hip_runtime.h>
#include <hip/hip_bf16.h>

constexpr int K_DI = 2;
constexpr int K_DP = 5;
constexpr int K_DF = 6;
constexpr int K_SV = 3450;
constexpr int K_BIAS = 8;
constexpr int K_D0 = 1376;
constexpr int K_GAIN = 5;
constexpr int K_GAIN2 = 7;
constexpr int K_DMIN = 5;
constexpr int K_DMIN2 = 4;
constexpr int K_TG2 = 3;
constexpr int K_M2 = 4;
constexpr int K_END = 6;
constexpr int K_FREE = 2;
constexpr unsigned long long K_PERM = 0xa9864573210ull;
constexpr int K_QDUE = 360;
constexpr int K_HDUE = 1000;
constexpr int K_WPS = 3;
constexpr int K_WT = 16;
constexpr int K_OT = 4;
constexpr int K_BIAS0 = 30;
constexpr int K_BIAS1 = 0;
constexpr int K_TDUE = 66;

namespace {
typedef unsigned char u8;
typedef unsigned u32;
typedef unsigned long long u64;
typedef u32 u32x2 __attribute__((ext_vector_type(2)));
typedef u32 u32x4 __attribute__((ext_vector_type(4)));
typedef u32 u32x8 __attribute__((ext_vector_type(8)));
typedef u32 u32x16 __attribute__((ext_vector_type(16)));
typedef float f32x2 __attribute__((ext_vector_type(2)));
typedef float f32x4 __attribute__((ext_vector_type(4)));
typedef __attribute__((ext_vector_type(2))) __bf16 bf16x2;
typedef __amdgpu_buffer_rsrc_t rsrc_t;
typedef __attribute__((address_space(3))) volatile u64 lds_vu64;
typedef __attribute__((address_space(3))) volatile u32 lds_vu32;

constexpr int DM = 512, FF = 1792;
constexpr int REGION = 2048 * 256;
constexpr int WS_EPOCH = 0, WS_ML = 4096, WS_PR = 40960, WS_QB = 65536, WS_HB = WS_QB + 128 * 1024, WS_GB = WS_HB + 64 * 1024, WS_TB = WS_GB + 64 * 1024,
              WS_BYTES = WS_TB + 256 * 2048;
constexpr u32 SENT = 0xFFFFFFFFu;
constexpr int SC1 = 16, NT = 2;
constexpr int SPIN_CAP = 1 << 18;
constexpr int L_Q = 0, L_OP = 64, L_Z = 896, L_ML = 576, L_RDY = 584, L_TRDY = 585, L_T0 = 586, L_CNT = 587, L_MK = 588, L_TAG = 604, L_WT = 608, L_P = 1216, L_H = L_P + 2 * 256, L_END = L_H + 2 * 256;

__device__ __forceinline__ rsrc_t mk(const void* p, int bytes) { return __builtin_amdgcn_make_buffer_rsrc((void*)p, 0, bytes, 0x00020000); }
template <int AUX> __device__ __forceinline__ u32x4 ld16(rsrc_t r, int voff, int soff) { return __builtin_amdgcn_raw_buffer_load_b128(r, voff, soff, AUX); }
template <int AUX> __device__ __forceinline__ u32x2 ld8(rsrc_t r, int voff, int soff) { return __builtin_amdgcn_raw_buffer_load_b64(r, voff, soff, AUX); }
template <int AUX> __device__ __forceinline__ u32 ld4(rsrc_t r, int voff, int soff) { return __builtin_amdgcn_raw_buffer_load_b32(r, voff, soff, AUX); }
template <int AUX> __device__ __forceinline__ u32 ld2(rsrc_t r, int voff, int soff) { return __builtin_amdgcn_raw_buffer_load_b16(r, voff, soff, AUX); }
__device__ __forceinline__ float asf(u32 x) { return __builtin_bit_cast(float, x); }
__device__ __forceinline__ u32 asu(float x) { return __builtin_bit_cast(u32, x); }
__device__ __forceinline__ float lo16(u32 x) { return asf(x << 16); }
__device__ __forceinline__ float hi16(u32 x) { return asf(x & 0xffff0000u); }
__device__ __forceinline__ u32 pk(float a, float b) { f32x2 v = {a, b}; return __builtin_bit_cast(u32, __builtin_convertvector(v, bf16x2)); }
__device__ __forceinline__ float rbf(float a) { return lo16(pk(a, a)); }
__device__ __forceinline__ float dot2(u32 a, u32 b, float c) {
  return __builtin_amdgcn_fdot2_f32_bf16(__builtin_bit_cast(bf16x2, a), __builtin_bit_cast(bf16x2, b), c, false);
}
template <int CTRL> __device__ __forceinline__ u32 dppu(u32 x) { return (u32)__builtin_amdgcn_update_dpp(0, (int)x, CTRL, 0xf, 0xf, true); }
template <int CTRL> __device__ __forceinline__ float dpp(float x) { return asf(dppu<CTRL>(asu(x))); }
constexpr int X1 = 0xB1, X2 = 0x4E, X7 = 0x141, X8 = 0x128, ROR4 = 0x124, BC = 0x150;
__device__ __forceinline__ float tr32(float a, float b) { auto s = __builtin_amdgcn_permlane32_swap(asu(a), asu(b), false, false); return asf(s[0]) + asf(s[1]); }
__device__ __forceinline__ float tr16(float a, float b) { auto s = __builtin_amdgcn_permlane16_swap(asu(a), asu(b), false, false); return asf(s[0]) + asf(s[1]); }
template <int CTRL> __device__ __forceinline__ float trd(float a, float b, bool up) { const float keep = up ? b : a, send = up ? a : b; return keep + dpp<CTRL>(send); }
__device__ __forceinline__ float wave_sum(float x) {
  x += dpp<X1>(x); x += dpp<X2>(x); x += dpp<X7>(x); x += dpp<X8>(x);
  x = tr16(x, x); return tr32(x, x);
}
__device__ __forceinline__ float wave_max(float x) {
  x = fmaxf(x, dpp<X1>(x)); x = fmaxf(x, dpp<X2>(x)); x = fmaxf(x, dpp<X7>(x)); x = fmaxf(x, dpp<X8>(x));
  auto s = __builtin_amdgcn_permlane16_swap(asu(x), asu(x), false, false); x = fmaxf(asf(s[0]), asf(s[1]));
  auto t = __builtin_amdgcn_permlane32_swap(asu(x), asu(x), false, false); return fmaxf(asf(t[0]), asf(t[1]));
}
template <int N> __device__ __forceinline__ void wait_vm() { __builtin_amdgcn_s_waitcnt((N & 15) | (7 << 4) | (15 << 8) | ((N >> 4) << 14)); }
#define WENT(n) "s_waitcnt vmcnt(" #n ")\n\ts_setpc_b64 s[76:77]\n\t"
__device__ __forceinline__ u64 wait_table() {
  u64 base;
  asm volatile("s_getpc_b64 %0\n.Lw_tab:\n\ts_branch .Lw_skip\n\t" WENT(0) WENT(1) WENT(2) WENT(3) WENT(4) WENT(5) WENT(6) WENT(7)
               "s_nop 0\n\ts_setpc_b64 s[76:77]\n.Lw_skip:" : "=s"(base));
  return base + 4 - 8;
}
__device__ __forceinline__ u64 wait_table_ref() {
  u64 pc;
  int off;
  asm volatile("s_getpc_b64 %0\n.Lw_ref:\n\ts_mov_b32 %1, .Lw_tab-.Lw_ref" : "=s"(pc), "=s"(off));
  return pc + off + 4 - 8;
}
__device__ __forceinline__ void dyn_wait(u64 fn) {
  asm volatile("s_swappc_b64 s[76:77], %0" :: "s"(fn) : "s76", "s77");
  __builtin_amdgcn_sched_barrier(0);
}
__device__ __forceinline__ int sel(bool p, int a, int b) { return p ? a : b; }
__device__ __forceinline__ u32 now() { return (u32)__builtin_amdgcn_s_memrealtime(); }
__device__ __forceinline__ int window(int lag, bool late) {
  int dd = max((K_D0 + lag * (late ? K_GAIN2 : K_GAIN)) >> 8, late ? K_DMIN2 : K_DMIN);
  asm("" : "+s"(dd));
  return min(dd, 8);
}
__device__ __forceinline__ void lds_done() { asm volatile("s_waitcnt lgkmcnt(0)" ::: "memory"); }
__device__ __forceinline__ void wg_barrier() { asm volatile("s_waitcnt lgkmcnt(0)\n\ts_barrier" ::: "memory"); }
#define SB() __builtin_amdgcn_sched_barrier(0)
__device__ __forceinline__ u32 nosent(float v) { const u32 u = asu(v); return u == SENT ? 0x7FC00000u : u; }

__device__ __forceinline__ float grp_sum(float x) { x += dpp<X1>(x); x += dpp<X2>(x); x = tr16(x, x); return tr32(x, x); }
__device__ __forceinline__ void ln_stats(const float (&e)[32], float& mb, float& inv) {
  float s0 = 0.f, s1 = 0.f;
#pragma unroll
  for (int i = 0; i < 32; i += 2) { s0 += e[i]; s1 += e[i + 1]; }
  const float mean = grp_sum(s0 + s1) * (1.f / DM);
  float q0 = 0.f, q1 = 0.f;
#pragma unroll
  for (int i = 0; i < 32; i += 2) { const float d0 = e[i] - mean, d1 = e[i + 1] - mean; q0 = fmaf(d0, d0, q0); q1 = fmaf(d1, d1, q1); }
  const float var = grp_sum(q0 + q1) * (1.f / DM);
  mb = rbf(mean);
  inv = 1.f / rbf(sqrtf(rbf(rbf(var) + 1e-5f)));
}
__device__ __forceinline__ u32 ln_pair(u32 v, float mb, float inv) {
  const u32 d = pk(lo16(v) - mb, hi16(v) - mb);
  return pk(lo16(d) * inv, hi16(d) * inv);
}
__device__ __forceinline__ u32x4 ln_vec(u32x4 v, float mb, float inv) { return u32x4{ln_pair(v[0], mb, inv), ln_pair(v[1], mb, inv), ln_pair(v[2], mb, inv), ln_pair(v[3], mb, inv)}; }
__device__ __forceinline__ void unpack32(const u32x4 (&v)[4], float (&e)[32]) {
#pragma unroll
  for (int i = 0; i < 16; ++i) { e[2 * i] = lo16(v[i >> 2][i & 3]); e[2 * i + 1] = hi16(v[i >> 2][i & 3]); }
}
typedef __attribute__((ext_vector_type(8))) __bf16 bf16x8;
__device__ __forceinline__ f32x4 mfma(u32x4 a, u32x4 b, f32x4 c) {
  return __builtin_amdgcn_mfma_f32_16x16x32_bf16(__builtin_bit_cast(bf16x8, a), __builtin_bit_cast(bf16x8, b), c, 0, 0, 0);
}
typedef __attribute__((ext_vector_type(16))) __bf16 bf16x16;
__device__ __forceinline__ f32x4 mfma64(u32x4 a, u32x8 b, f32x4 c, int idx) {
  return __builtin_amdgcn_smfmac_f32_16x16x64_bf16(__builtin_bit_cast(bf16x8, a), __builtin_bit_cast(bf16x16, b), c, idx, 0, 0);
}
__device__ __forceinline__ u32 plo(u32 a, u32 b) { return __builtin_amdgcn_perm(b, a, 0x05040100); }
__device__ __forceinline__ u32 phi(u32 a, u32 b) { return __builtin_amdgcn_perm(b, a, 0x07060302); }
template <int NS> __device__ __forceinline__ void gemv(const u32x2 (&W)[8 * NS], const u32x4 (&a)[NS], f32x4 (&acc)[4]) {
#pragma unroll
  for (int s = 0; s < NS; ++s)
#pragma unroll
    for (int jj = 0; jj < 4; ++jj) {
      u32x4 f;
#pragma unroll
      for (int p = 0; p < 4; ++p) { const u32 x0 = W[8 * s + 2 * p][jj >> 1], x1 = W[8 * s + 2 * p + 1][jj >> 1]; f[p] = (jj & 1) ? phi(x0, x1) : plo(x0, x1); }
      acc[jj] = mfma(a[s], f, acc[jj]);
    }
}
__device__ __forceinline__ void st16(u32x4 u, rsrc_t r, int voff, int soff) {
  __builtin_amdgcn_raw_buffer_store_b128(u, r, voff, soff, SC1);
  asm volatile("s_nop 1" :: "v"(u) : "memory");
}
__device__ __forceinline__ void put4(rsrc_t r, float v0, float v1, float v2, float v3, u32 tag, int voff, int soff) {
  const u32x4 u = {__builtin_amdgcn_perm(asu(v0), tag, 0x07060500), __builtin_amdgcn_perm(asu(v1), tag, 0x07060501),
                   __builtin_amdgcn_perm(asu(v2), tag, 0x07060502), __builtin_amdgcn_perm(asu(v3), tag, 0x07060503)};
  st16(u, r, voff, soff);
}
__device__ __forceinline__ float val(u32 u) { return asf(u & 0xffffff00u); }
__device__ __forceinline__ u32 tag_of(u32x4 v) {
  return __builtin_amdgcn_perm(__builtin_amdgcn_perm(v[3], v[2], 0x04000400), __builtin_amdgcn_perm(v[1], v[0], 0x04000400), 0x05040100);
}
template <int N, int SLEEP, class F> __device__ __forceinline__ void gather(u32x4 (&v)[N], u32 tag, F&& load) {
#pragma unroll 1
  for (int spin = 0; spin < SPIN_CAP; ++spin) {
    asm volatile("" ::: "memory");
    bool ok = true;
#pragma unroll
    for (int i = 0; i < N; ++i) v[i] = load(i);
#pragma unroll
    for (int i = 0; i < N; ++i) ok = ok && tag_of(v[i]) == tag;
    if (__all(ok)) break;
    if (SLEEP) __builtin_amdgcn_s_sleep(SLEEP);
  }
  asm volatile("" ::: "memory");
}
__device__ __forceinline__ float box_reduce(float t0, float t1, float t2, float t3) {
  float z = tr16(tr32(t0, t2), tr32(t1, t3));
  z += dpp<X8>(z); z += dpp<ROR4>(z); z += dpp<X2>(z);
  return z;
}
__device__ __forceinline__ void interleave(u32x4 a, u32x4 b, u32x4& lo, u32x4& hi) {
  lo[0] = __builtin_amdgcn_perm(b[0], a[0], 0x05040100); lo[1] = __builtin_amdgcn_perm(b[0], a[0], 0x07060302);
  lo[2] = __builtin_amdgcn_perm(b[1], a[1], 0x05040100); lo[3] = __builtin_amdgcn_perm(b[1], a[1], 0x07060302);
  hi[0] = __builtin_amdgcn_perm(b[2], a[2], 0x05040100); hi[1] = __builtin_amdgcn_perm(b[2], a[2], 0x07060302);
  hi[2] = __builtin_amdgcn_perm(b[3], a[3], 0x05040100); hi[3] = __builtin_amdgcn_perm(b[3], a[3], 0x07060302);
}
template <int I> struct IC { static constexpr int v = I; };
template <int N, int I = 0, class F> __device__ __forceinline__ void sfor(F&& f) {
  if constexpr (I < N) { f(IC<I>{}); sfor<N, I + 1>(f); }
}

struct KTree { float x1, x2, x4, x8, out; };
__device__ __forceinline__ float k_dot(const u32x4 k, const u32x4 q) {
  float a = dot2(k[0], q[0], 0.f);
  a = dot2(k[1], q[1], a); a = dot2(k[2], q[2], a);
  return dot2(k[3], q[3], a);
}
template <int I> __device__ __forceinline__ void k_merge(KTree& t, float a, int c) {
  if constexpr (!(I & 1)) { t.x1 = a; return; }
  a = trd<X8>(t.x1, a, c & 8);
  if constexpr (!(I & 2)) { t.x2 = a; return; }
  a = trd<X7>(t.x2, a, c & 4);
  if constexpr (!(I & 4)) { t.x4 = a; return; }
  a = trd<X2>(t.x4, a, c & 2);
  if constexpr (!(I & 8)) { t.x8 = a; return; }
  t.out = rbf(rbf(trd<X1>(t.x8, a, c & 1)) * 0.08838834764831845f);
}
constexpr int kbit(int k) { return 256 << ((K_PERM >> (4 * k)) & 15); }
constexpr int kvo(int n) { int o = 0; for (int k = 0; k < 7; ++k) o |= ((n >> k) & 1) * kbit(4 + k); return o; }
constexpr int KLW = kbit(0) | kbit(1) | kbit(2) | kbit(3);
constexpr int bitrev4(int i) { return ((i & 1) << 3) | ((i & 2) << 1) | ((i & 4) >> 1) | ((i & 8) >> 3); }
template <int I> __device__ __forceinline__ void pv_step(f32x2 (&acc)[4], float p, const u32x4 v) {
  const float pb = dpp<BC + bitrev4(I)>(p);
#pragma unroll
  for (int k = 0; k < 4; ++k) acc[k] = __builtin_elementwise_fma(f32x2{pb, pb}, f32x2{lo16(v[k]), hi16(v[k])}, acc[k]);
  asm volatile("" : "+v"(acc[0]), "+v"(acc[1]), "+v"(acc[2]), "+v"(acc[3]));
}
__device__ __forceinline__ void pin(float& x) { asm volatile("" : "+v"(x)); }

__global__ void __launch_bounds__(512, 2) __attribute__((aligned(4096)))
fused_decode(const u8*  , const u8*  , const u8*  , const u8*  , const u8*  , const u8*  , const u8*  , const u8*  ,
             const u8*  , u8*  , u8*  ) {
  __shared__ __attribute__((aligned(16))) float lds[L_END];
  const auto kargs = __builtin_amdgcn_kernarg_segment_ptr();
  u32x16 ka;
  u32x4 kb;
  u32x2 kc2;
  asm volatile("s_load_dwordx16 %0, %3, 0x0\n\ts_load_dwordx4 %1, %3, 0x40\n\ts_load_dwordx2 %2, %3, 0x50" : "=&s"(ka), "=&s"(kb), "=&s"(kc2) : "s"(kargs));
  SB();
  const int lane = threadIdx.x & 63, w = __builtin_amdgcn_readfirstlane(threadIdx.x >> 6);
  const int r = lane >> 4, c = lane & 15;
  const int g = blockIdx.x, x8 = g & 7, j = g >> 3;
  const int h = g & 31, bs = g >> 5, b = bs >> 1, split = bs & 1;

  u32 code;
  {
    u64 pc;
    int len;
    asm volatile("s_getpc_b64 %0\n.Lk_here:\n\ts_mov_b32 %1, ((.Lk_end-.Lk_here)+4095)&0xfffff000" : "=s"(pc), "=s"(len));
    code = ld4<0>(mk((const u8*)(pc & ~(u64)4095), sel(w == 7 && len > 0, len, 0)), lane * 32 + j * 2048, 0);
    asm volatile("" ::: "memory");
    SB();
  }
  const int tk = ((4 * (w + b) + (j & 3)) & 15) * 4;
  u64 tp;
  asm volatile("s_load_dwordx2 %0, %1, %2" : "=&s"(tp)
               : "s"(kargs), "s"(8 * (sel(w < 4, (int)(0x75033986754433a0ull >> tk), 0x44447533u >> (8 * (w & 2) + 4 * b)) & 15)));
  SB();
  static_assert(KLW == 0xf00 && kbit(0) == 256 && kbit(1) == 512 && kbit(2) == 1024, "voff below");
  const int hw = w - 4, cl = split * 4 + (h >> 3);
  const int cg = x8 * 8 + (j & 3) * 2 + split, col0 = 8 * cg;
  int so, sz, pitch, km, vo;
  u64 rbase;
  if (w < 4) {
    so = cg * 16 + ((int)(0x0004000000404030ull >> tk) & 15) * 524288 + sel(tk == 40, b * 1024, 0);
    sz = sel(split, so + 4, 0);
    km = (j & 7) << 12;
    rbase = (u64)(u32)((b * 32 + h) * 2 + split) * REGION;
    vo = threadIdx.x * 16;
    asm volatile("" : "+s"(so), "+s"(sz), "+s"(km), "+s"(rbase), "=s"(pitch));
    asm volatile("" : "+v"(vo));
  } else {
    pitch = sel(b < 2, 8192, sel(b == 2, FF * 2, 1024));
    so = ((hw + 2 * sel(b < 2, b, sel(b == 2, split, x8))) * pitch + sel(b < 2, 2 * (h ^ 1) + split, sel(b == 2, h, cl))) * 128;
    sz = sel(b < 2, DM * 4096 * 2, sel(sel(b == 2, h, 4 * x8) < 28, DM * FF * 2, 0));
    vo = r * 8 * pitch + c * 8;
    asm volatile("" : "+s"(so), "+s"(sz), "=s"(km), "=s"(rbase), "+s"(pitch));
    asm volatile("" : "+v"(vo));
  }
  asm volatile("s_waitcnt lgkmcnt(0)" : "+s"(ka), "+s"(kb), "+s"(kc2), "+s"(tp), "+s"(so), "+s"(sz), "+s"(km), "+s"(rbase), "+s"(pitch));
  auto ptr = [](u32 lo, u32 hi) { return (u8*)(((u64)hi << 32) | lo); };
  const u8 *x = ptr(ka[0], ka[1]), *kc = ptr(ka[2], ka[3]), *vc = ptr(ka[4], ka[5]), *wo = ptr(ka[8], ka[9]), *b1 = ptr(ka[12], ka[13]), *b2 = ptr(kb[0], kb[1]);
  u8 *out = ptr(kb[2], kb[3]), *ws = ptr(kc2[0], kc2[1]);
  u32x4 A[16];
  u32 touch;
  int ws_ = w;
  if (ws_ < 4) {
    touch = ld4<NT>(mk((const u8*)tp, sz), 0, so);
    SB();
#pragma unroll
    for (int i = 0; i < K_DI; ++i) A[i] = ld16<NT>(mk(kc + rbase, REGION), vo, kvo(i) ^ km);
    SB();
    if (threadIdx.x < 28) ((u32*)lds)[L_ML + threadIdx.x] = threadIdx.x < 8 ? SENT : 0u;
    wg_barrier();
  } else {
    asm volatile("" : "=v"(touch));
#pragma unroll
    for (int i = 0; i < K_DI; ++i) asm volatile("" : "=v"(A[i]));
  }
  asm volatile("" : "+s"(ws_));
  const rsrc_t rx = mk(x, 4 * DM * 2), rws = mk(ws, WS_BYTES);
  if (ws_ < 4) {
    const rsrc_t rk = mk(kc + rbase, REGION);
    const int voff = vo;
    u32x4 Bq[16], Cq[16];
    __builtin_amdgcn_s_setprio(1);
    const u64 wtab = wait_table();
#pragma unroll
    for (int i = K_DI; i < 16; ++i) {
      wait_vm<K_DP - 1>();
      A[i] = ld16<NT>(rk, voff, kvo(i) ^ km); SB();
    }
    asm volatile("" :: "v"(touch), "v"(code));
    u32 rdy = 0;
    u32x4 q = {};
    auto read_q = [&]() {
      rdy = *(lds_vu32*)(lds + L_RDY);
#pragma unroll
      for (int k = 0; k < 2; ++k) { const u64 v = *(lds_vu64*)(lds + L_Q + 4 * c + 2 * k); q[2 * k] = (u32)v; q[2 * k + 1] = (u32)(v >> 32); }
    };
#pragma unroll
    for (int i = 0; i < 16; ++i) { wait_vm<K_DP - 1>(); Bq[i] = ld16<NT>(rk, voff, kvo(16 + i) ^ km); SB(); }
#pragma unroll
    for (int i = 0; i < 16; ++i) {
      if (i == 15) { read_q(); SB(); }
      wait_vm<K_DP - 1>(); Cq[i] = ld16<NT>(rk, voff, kvo(32 + i) ^ km); SB();
    }
#pragma unroll 1
    for (int spin = 0; spin < SPIN_CAP * 16 && __builtin_amdgcn_readfirstlane(rdy) == 0u; ++spin) read_q();
    int d = K_DF;
    u64 wfn = wtab + 8 * d;
    float s[8], m;
#pragma unroll
    for (int i = 0; i < 8; ++i) s[i] = 0.f;
    auto blk = [&](int t) { return ((t & 1) * kbit(8) | ((t >> 1) & 1) * kbit(9) | (t >> 2) * kbit(10)) ^ km; };
    u32 tn = 0, t1 = 0, own = 0;
#define WINDOW(n0, mark) \
    if constexpr (i == 2) { tn = now(); t1 = *(lds_vu32*)(lds + L_T0); } \
    if constexpr (i == 3) { \
      const u32 me = tn - ((((n0) + 2 - d) * K_SV) >> 8);     \
      if ((mark) >= 0) { if ((mark) == 0) own = me - K_BIAS; if (lane == 0) *(lds_vu32*)(lds + L_MK + 4 * (mark) + w) = me | 1u; } \
      const u32 t0 = __builtin_amdgcn_readfirstlane(t1); \
      d = window((int)(me - (t0 ? t0 : own)), (n0) >= 147 + 16 * K_TG2); \
      wfn = wtab + 8 * d; \
    }
    {
      KTree ta, tb, tc;
      sfor<16>([&](auto I) {
        constexpr int i = I.v;
        WINDOW(48, 0)
        float a = k_dot(A[i], q);
        pin(a);
        dyn_wait(wfn);
        A[i] = ld16<NT>(rk, voff, kvo(48 + i) ^ km);
        SB();
        k_merge<i>(ta, a, c);
        k_merge<i>(tb, k_dot(Bq[i], q), c);
        k_merge<i>(tc, k_dot(Cq[i], q), c);
      });
      s[0] = ta.out; s[1] = tb.out; s[2] = tc.out;
      m = fmaxf(fmaxf(ta.out, tb.out), tc.out);
    }
#pragma unroll 1
    for (int t = 3; t < 8; ++t) {
      const bool last = t == 7;
      const rsrc_t nxt = mk((last ? vc : kc) + rbase, REGION);
      const int soff = blk(last ? 0 : t + 1);
      KTree ta;
      sfor<16>([&](auto I) {
        constexpr int i = I.v;
        WINDOW(16 * (t + 1), last ? 1 : -1)
        float a = k_dot(A[i], q);
        pin(a);
        dyn_wait(wfn);
        A[i] = ld16<NT>(nxt, voff, kvo(i) ^ soff);
        SB();
        k_merge<i>(ta, a, c);
      });
#pragma unroll
      for (int i = 3; i < 7; ++i) s[i] = s[i + 1];
      s[7] = ta.out;
      m = fmaxf(m, ta.out);
    }
    const rsrc_t rv = mk(vc + rbase, REGION);
    u32x4 X[3];
    pin(m);
    dyn_wait(wfn); X[0] = ld16<NT>(rv, voff, kvo(125) ^ km); SB();
    m = wave_max(m);
    pin(m);
    dyn_wait(wfn); X[1] = ld16<NT>(rv, voff, kvo(126) ^ km); SB();
    float l = 0.f;
#pragma unroll
    for (int i = 0; i < 8; ++i) { s[i] = __builtin_amdgcn_exp2f((s[i] - m) * 1.4426950408889634f); l += s[i]; }
    pin(l);
    dyn_wait(wfn); X[2] = ld16<NT>(rv, voff, kvo(127) ^ km); SB();
    l = wave_sum(l);
    if (lane == 0) *(lds_vu64*)(lds + L_ML + 2 * w) = (u64)asu(m) | ((u64)nosent(l) << 32);
    f32x2 acc[4] = {};
    sfor<K_END>([&](auto I) {
      constexpr int i = I.v;
      WINDOW(147, -1)
      pv_step<i>(acc, s[0], A[i]);
      dyn_wait(wfn);
      A[i] = ld16<NT>(rv, voff, kvo(16 + i) ^ km);
      SB();
      if constexpr (i >= K_END - 3) pv_step<i + 16 - K_END>(acc, s[7], X[i + 3 - K_END]);
    });
#pragma unroll 1
    for (int t = 1; t < 7; ++t) {
      const int soff = blk(t), soff1 = blk(t + 1);
      sfor<16, K_END>([&](auto I) {
        constexpr int i = I.v;
        pv_step<i>(acc, s[0], A[i]);
        dyn_wait(wfn);
        A[i] = ld16<NT>(rv, voff, kvo(i) ^ soff);
        SB();
      });
#pragma unroll
      for (int i = 0; i < 7; ++i) s[i] = s[i + 1];
      sfor<K_END>([&](auto I) {
        constexpr int i = I.v;
        WINDOW(147 + 16 * t, t == K_M2 ? 2 : -1)
        pv_step<i>(acc, s[0], A[i]);
        dyn_wait(wfn);
        A[i] = ld16<NT>(rv, voff, kvo(i) ^ soff1);
        SB();
      });
    }
    constexpr int NY = 13 - K_END;
    u32x4 Y[NY];
    int op_at = (L_OP + 144 * (c >> 3) + 72 * (c & 1) + 16 * w + 8 * ((c >> 2) & 1) + 2 * r + ((c >> 1) & 1)) * 4;
    asm volatile("" : "+v"(op_at));
    auto tail = [&](auto N) {
      constexpr int n = N.v, i = (K_END + n) & 15;
      if constexpr (n < 16 - K_END) pv_step<i>(acc, s[0], A[i]);
      else if constexpr (n < 16) pv_step<i>(acc, s[7], A[i]);
      else pv_step<i>(acc, s[7], Y[n - 16]);
      if constexpr ((n & 1) && n / 2 < NY) {
        if constexpr (n / 2 < NY - K_FREE) dyn_wait(wfn);
        Y[n / 2] = ld16<NT>(rv, voff, kvo(112 + K_END + n / 2) ^ km);
      }
      SB();
    };
    sfor<16 + NY>(tail);
    *(u32*)((u8*)lds + op_at) = pk(tr16(tr32(acc[0][0], acc[1][0]), tr32(acc[0][1], acc[1][1])), tr16(tr32(acc[2][0], acc[3][0]), tr32(acc[2][1], acc[3][1])));
    wg_barrier();
    asm volatile(".Lk_end:");
  } else {
    auto wait_flag = [&](int word) {
#pragma unroll 1
      for (int spin = 0; spin < SPIN_CAP * 16 && __builtin_amdgcn_readfirstlane(*(lds_vu32*)(lds + word)) == 0u; ++spin) __builtin_amdgcn_s_sleep(4);
      asm volatile("" ::: "memory");
    };
    __builtin_amdgcn_s_setprio(2);
    u32x2 W[32];
    int wave2 = hw >> 1;
    if (wave2) {
      if (K_WPS) __builtin_amdgcn_s_sleep(K_WPS);
      const rsrc_t ro = mk((const u8*)tp, 4096 * DM * 2);
      u32x4 t[8];
#pragma unroll
      for (int k = 0; k < 8; ++k) {
        if (k >= K_OT) wait_vm<K_OT - 1>();
        t[k] = ld16<0>(ro, lane * 16 + (k & 3) * 1024, h * 131072 + bs * 16384 + (hw - 2) * 8192 + (k >> 2) * 4096);
        SB();
      }
      wg_barrier();
#pragma unroll
      for (int k = 0; k < 8; ++k) asm volatile("" :: "v"(t[k]));
    }
    asm volatile("" : "+s"(wave2));
    const int role = sel(sz == 0, 3, max(b - 1, 0));
    u32 ep = 0, t_h = 0;
    auto put_tag = [&]() { if (hw == 0 && lane == 0) { ((u32*)lds)[L_TAG] = ep + 0x01010101u; lds_done(); *(lds_vu32*)(lds + L_TRDY) = 1u; } };
    auto until = [&](u32 due) {
#pragma unroll 1
      for (int spin = 0; spin < 64 && (int)(now() - due) < 0; ++spin) __builtin_amdgcn_s_sleep(8);
    };
    auto get_q = [&]() {
      const u32 tag = __builtin_amdgcn_readfirstlane(ep) + 0x01010101u;
      until(t_h + K_QDUE);
      u32x4 v[1];
      gather<1, 16>(v, tag, [&](int) { return ld16<SC1>(rws, r * 1024 + c * 16, WS_QB + h * 4096 + b * 256); });
      const float z0 = tr32(val(v[0][0]), val(v[0][2])), z1 = tr32(val(v[0][1]), val(v[0][3]));
      ((u32*)lds)[L_Q + 2 * (lane & 31) + (lane >> 5)] = pk(z0, z1);
      lds_done();
      if (lane == 0) *(lds_vu32*)(lds + L_RDY) = 1u;
    };
    if (!wave2) {
      const rsrc_t rw = mk((const u8*)tp, sz);
#pragma unroll
      for (int i = 0; i < 32; ++i) { if (i >= K_WT) wait_vm<K_WT - 1>(); W[i] = ld8<NT>(rw, vo, so + (32 * (i >> 3) + (i & 7)) * pitch); SB(); }
      const u64 t64 = __builtin_amdgcn_s_memrealtime();
      wg_barrier();
      t_h = (u32)t64;
      ep = ld4<SC1>(mk(ws, sel(hw == 0, 4, 0)), WS_EPOCH, 0);
      const rsrc_t rxx = mk(x, sel(role < 2, 4 * DM * 2, 0));
      const int xs_off = (c >> 2) * 1024 + (r * 4 + (c & 3)) * 16;
      const int xa_off = (c >> 2) * 1024 + r * 16 + hw * 256 + sel(b < 2, b, split) * 512;
      u32x4 xs[4], xa[4];
#pragma unroll
      for (int i = 0; i < 4; ++i) xs[i] = ld16<0>(rxx, xs_off + i * 256, 0);
#pragma unroll
      for (int i = 0; i < 4; ++i) xa[i] = ld16<0>(rxx, xa_off + i * 64, 0);
      SB();
      __builtin_amdgcn_s_setprio(0);
      put_tag();
      if (role != 3) {
        if (role != 2) {
#pragma unroll 1
          for (int n = 0; n <= role; ++n) {
            float e[32], mb, inv;
            unpack32(xs, e);
            ln_stats(e, mb, inv);
#pragma unroll
            for (int i = 0; i < 4; ++i) xa[i] = ln_vec(xa[i], mb, inv);
            if (n < role) {
#pragma unroll
              for (int i = 0; i < 4; ++i) xs[i] = ln_vec(xs[i], mb, inv);
            }
          }
        }
        wait_flag(L_TRDY);
        const u32 tag = __builtin_amdgcn_readfirstlane(((const u32*)lds)[L_TAG]);
        if (role == 2) {
          u32x2 bias[2];
#pragma unroll
          for (int i = 0; i < 2; ++i) bias[i] = ld8<0>(mk(b1, FF * 2), c * 8 + i * 128, x8 * 512 + hw * 256);
          if (hw == 0) get_q();
          until(t_h + K_HDUE);
          u32x4 v[4];
          gather<4, 32>(v, tag, [&](int i) { return ld16<SC1>(rws, lane * 16 + i * 1024, WS_HB + (8 * x8 + 4 * hw) * 1024); });
#pragma unroll
          for (int u = 0; u < 2; ++u) {
            float z[4];
#pragma unroll
            for (int k = 0; k < 4; ++k) {
              const float t = rbf(rbf(val(v[2 * u][k]) + val(v[2 * u + 1][k])) + ((k & 1) ? hi16(bias[u][k >> 1]) : lo16(bias[u][k >> 1])));
              const float uu = 0.7978845608028654f * (t + 0.044715f * t * t * t);
              z[k] = t * __builtin_amdgcn_rcpf(1.f + __expf(-2.f * uu));
            }
            *(u32x2*)(lds + L_H + hw * 256 + r * 64 + u * 32 + c * 2) = u32x2{pk(z[0], z[1]), pk(z[2], z[3])};
          }
          lds_done();
#pragma unroll
          for (int i = 0; i < 4; ++i) xa[i] = *(const u32x4*)(lds + L_H + hw * 256 + (c >> 2) * 64 + i * 16 + r * 4);
        }
        f32x4 acc[4] = {};
        gemv<4>(W, xa, acc);
        *(f32x4*)(lds + L_P + hw * 256 + lane * 4) = f32x4{acc[0][0], acc[1][0], acc[2][0], acc[3][0]};
        u32 seen = 0;
        lds_done();
        if (lane == 0) seen = __hip_atomic_fetch_add((__attribute__((address_space(3))) u32*)(lds + L_CNT), 1u, __ATOMIC_RELAXED, __HIP_MEMORY_SCOPE_WORKGROUP);
        asm volatile("" ::: "memory");
        if (__builtin_amdgcn_readfirstlane(seen) == 1u) {
          const f32x4 z = *(const f32x4*)(lds + L_P + lane * 4) + *(const f32x4*)(lds + L_P + 256 + lane * 4);
          put4(rws, z[0], z[1], z[2], z[3], tag, lane * 16, sel(role == 0, WS_QB + (4 * (h ^ 1) + bs) * 1024, sel(role == 1, WS_HB + (2 * h + split) * 1024, WS_GB + (cl * 8 + x8) * 1024)));
        }
      }
    }
    __builtin_amdgcn_s_setprio(0);
    if (hw == 0 && role != 2) get_q();
    asm volatile("" :: "v"(code));
    wait_flag(L_RDY);
    const u32 tag = __builtin_amdgcn_readfirstlane(((const u32*)lds)[L_TAG]);
    const int fcol = (lane & 1) * 4 + (lane >> 5) * 2 + (r & 1);
    auto centre = [&](int k) {
      u32 tw = 0;
#pragma unroll 1
      for (int spin = 0; spin < SPIN_CAP * 16; ++spin) {
        tw = *(lds_vu32*)(lds + L_MK + 4 * k + (lane & 3));
        if (__all(tw != 0u)) break;
        __builtin_amdgcn_s_sleep(8);
      }
      int dt = (int)(tw - dppu<0>(tw));
      dt += (int)dppu<X1>(dt); dt += (int)dppu<X2>(dt);
      tw = dppu<0>(tw) + (dt >> 2);
      if ((g >> 4) == (g & 15) && lane < 4) put4(rws, asf(tw << 8), 0.f, 0.f, 0.f, tag, lane * 16, WS_PR + (k * 16 + (g & 15)) * 128);
      if (k == 0) __builtin_amdgcn_s_sleep(90); else __builtin_amdgcn_s_sleep(64);
      u32x4 v[1];
      gather<1, 16>(v, tag, [&](int) { return ld16<SC1>(rws, c * 128 + k * 2048, WS_PR); });
      dt = (int)((v[0][0] & 0xffffff00u) - (tw << 8)) >> 8;
      dt += (int)dppu<X1>(dt); dt += (int)dppu<X2>(dt); dt += (int)dppu<X7>(dt); dt += (int)dppu<X8>(dt);
      if (lane == 0) *(lds_vu32*)(lds + L_T0) = (tw + (dt >> 4) - (k == 0 ? K_BIAS0 : k == 1 ? K_BIAS1 : K_BIAS)) | 1u;
    };
    if (hw == 2) centre(0);
    u32x8 Wf[16];
    {
      u32x4 Wt[32];
      const rsrc_t ro = mk(wo, 4096 * DM * 2);
      const u64 wfn = wait_table_ref() + 8 * (hw == 2 ? 7 : 3);
#pragma unroll
      for (int i = 0; i < 32; ++i) {
        dyn_wait(wfn);
        const u32x2 lo = ld8<0>(ro, r * 1024 + c * 8, (128 * h + 4 * i) * 1024 + hw * 256), hi = ld8<0>(ro, r * 1024 + c * 8 + 128, (128 * h + 4 * i) * 1024 + hw * 256);
        Wt[i] = u32x4{lo[0], lo[1], hi[0], hi[1]};
        SB();
      }
#pragma unroll
      for (int st = 0; st < 2; ++st) {
        u32x4 lo[8], hi[8];
#pragma unroll
        for (int p = 0; p < 8; ++p) interleave(Wt[16 * st + 2 * p], Wt[16 * st + 2 * p + 1], lo[p], hi[p]);
#pragma unroll
        for (int k = 0; k < 4; ++k) {
          Wf[8 * st + k] = u32x8{lo[0][k], lo[1][k], lo[2][k], lo[3][k], lo[4][k], lo[5][k], lo[6][k], lo[7][k]};
          Wf[8 * st + 4 + k] = u32x8{hi[0][k], hi[1][k], hi[2][k], hi[3][k], hi[4][k], hi[5][k], hi[6][k], hi[7][k]};
        }
#pragma unroll
        for (int k = 0; k < 8; ++k) asm volatile("" : "+v"(Wf[8 * st + k]));
      }
    }
    if (hw == 2) {
#pragma unroll 1
      for (int k = 1; k < 3; ++k) centre(k);
    }
    if (g == 0 && hw == 1) {
      wait_flag(L_MK + 8);
      u32x4 v[4];
      gather<4, 64>(v, tag, [&](int i) { return ld16<SC1>(rws, lane * 128 + i * 8192, WS_ML); });
      if (lane == 0) __builtin_amdgcn_raw_buffer_store_b32(tag, rws, WS_EPOCH, 0, SC1);
    }
    float gate = 0.f, xres = 0.f;
    if (hw == 0) {
      u32x2 ml = {0u, 0u};
#pragma unroll 1
      for (int spin = 0; spin < SPIN_CAP * 16; ++spin) {
        const u64 v = *(lds_vu64*)(lds + L_ML + 2 * (lane & 3));
        ml = u32x2{(u32)v, (u32)(v >> 32)};
        if (__all(ml[1] != SENT)) break;
        __builtin_amdgcn_s_sleep(8);
      }
      const float mw = asf(ml[0]);
      float M = fmaxf(mw, dpp<X1>(mw)); M = fmaxf(M, dpp<X2>(M));
      float L = asf(ml[1]) * __builtin_amdgcn_exp2f((mw - M) * 1.4426950408889634f);
      L += dpp<X1>(L); L += dpp<X2>(L);
      if (lane < 4) put4(rws, M, L, 0.f, 0.f, tag, lane * 16, WS_ML + (g ^ 32) * 128);
      __builtin_amdgcn_s_sleep(127);
      {
        const u32 bias2 = ld2<0>(mk(b2, DM * 2), fcol * 2, col0 * 2);
        const u32 xo = ld2<0>(rx, fcol * 2, b * 1024 + col0 * 2);
        u32x4 v[1];
        gather<1, 16>(v, tag, [&](int) { return ld16<SC1>(rws, (lane < 14 ? lane >> 1 : 0) * 1024 + (lane & 1) * 16, WS_GB + (col0 >> 6) * 8192 + b * 256 + (col0 & 63) * 4); });
        const bool on = lane < 14;
        float z = box_reduce(on ? val(v[0][0]) : 0.f, on ? val(v[0][1]) : 0.f, on ? val(v[0][2]) : 0.f, on ? val(v[0][3]) : 0.f);
        z = rbf(rbf(z) + lo16(bias2));
        gate = rbf(z / (1.f + __expf(-z)));
        xres = lo16(xo);
      }
      u32x4 ot[1];
      gather<1, 16>(ot, tag, [&](int) { return ld16<SC1>(rws, (lane & 3) * 16, WS_ML + g * 128); });
      const float Mo = val(ot[0][0]), Mt = fmaxf(M, Mo);
      const float Lt = L * __builtin_amdgcn_exp2f((M - Mt) * 1.4426950408889634f) + val(ot[0][1]) * __builtin_amdgcn_exp2f((Mo - Mt) * 1.4426950408889634f);
      if (lane < 4) lds[L_WT + lane] = __builtin_amdgcn_exp2f((mw - Mt) * 1.4426950408889634f) / Lt;
    }
    {
      if (hw == 3) { *(u32x4*)(lds + L_Z + 4 * lane) = u32x4{0u, 0u, 0u, 0u}; ((u32*)lds)[L_Z + 256 + lane] = 0u; }
      const int aoff = 72 * ((c >> 2) & 1) + 16 * (c & 3) + 4 * r;
      int alo = ((c < 8 ? L_OP : L_Z) + aoff) * 4, ahi = ((c >= 8 ? L_OP : L_Z) + aoff) * 4, idx = (c & 4) ? 0xeeee : 0x4444;
      int voff2 = (r & 1) ? WS_BYTES : r * 128 + c * 16, soff2 = WS_TB + (b * 64 + 2 * h + split) * 2048 + hw * 512;
      u32 psel[4] = {0x07060500u, 0x07060501u, 0x07060502u, 0x07060503u};
      f32x4 acc[4] = {};
      asm volatile("" : "+v"(alo), "+v"(ahi), "+v"(idx), "+v"(voff2), "+v"(psel[0]), "+v"(psel[1]), "+v"(psel[2]), "+v"(psel[3]), "+v"(acc[0]), "+v"(acc[1]),
                   "+v"(acc[2]), "+v"(acc[3]));
      asm volatile("" : "+s"(soff2));
      wg_barrier();
      u32x4 Af[4];
#pragma unroll
      for (int i = 0; i < 4; ++i) Af[i] = *(const u32x4*)((const u8*)lds + (i < 2 ? alo : ahi) + 576 * (i & 1));
      const f32x4 wt = *(const f32x4*)(lds + L_WT);
      SB();
#pragma unroll
      for (int i = 0; i < 4; ++i)
#pragma unroll
        for (int k = 0; k < 4; ++k) acc[k] = mfma64(Af[i], Wf[8 * (i & 1) + 4 * (i >> 1) + k], acc[k], idx);
      SB();
      f32x2 t[4];
      float z[4];
#pragma unroll
      for (int k = 0; k < 4; ++k) { t[k] = __builtin_elementwise_fma(acc[k].zw, wt.zw, acc[k].xy * wt.xy); asm volatile("" : "+v"(t[k])); }
#pragma unroll
      for (int k = 0; k < 4; ++k) { z[k] = t[k][0] + t[k][1]; pin(z[k]); }
      const float y0 = tr16(z[0], z[1]), y1 = tr16(z[2], z[3]);
      const auto s0 = __builtin_amdgcn_permlane16_swap(asu(y0), asu(y0), false, false), s1 = __builtin_amdgcn_permlane16_swap(asu(y1), asu(y1), false, false);
      const u32 u[4] = {__builtin_amdgcn_perm(s0[0], tag, psel[0]), __builtin_amdgcn_perm(s0[1], tag, psel[1]), __builtin_amdgcn_perm(s1[0], tag, psel[2]),
                        __builtin_amdgcn_perm(s1[1], tag, psel[3])};
      st16(u32x4{u[0], u[1], u[2], u[3]}, rws, voff2, soff2);
    }
    if (hw == 0) {
      int vin = (lane >> 1) * 2048 + (lane & 1) * 16, vout = c < 2 ? fcol * 2 : 4 * DM * 2;
      int sin0 = WS_TB + b * 64 * 2048 + cg * 32, sin1 = sin0 + 32 * 2048, sout = (b * DM + col0) * 2;
      u64 po = (u64)out;
      asm volatile("" : "+v"(vin), "+v"(vout));
      asm volatile("" : "+s"(sin0), "+s"(sin1), "+s"(sout), "+s"(po));
      const rsrc_t ro = mk((u8*)po, 4 * DM * 2);
      {
        const u32 due = __builtin_amdgcn_readfirstlane(*(lds_vu32*)(lds + L_T0)) + K_SV + K_TDUE;
#pragma unroll 1
        for (int spin = 0; spin < 16 && (int)(now() - due) < 0; ++spin) __builtin_amdgcn_s_sleep(1);
      }
      wait_vm<0>();
#pragma unroll 1
      for (int spin = 0; spin < SPIN_CAP; ++spin) {
        asm volatile("" ::: "memory");
        const u32x4 v0 = ld16<SC1>(rws, vin, sin0), v1 = ld16<SC1>(rws, vin, sin1);
        const float z = box_reduce(val(v0[0]) + val(v1[0]), val(v0[1]) + val(v1[1]), val(v0[2]) + val(v1[2]), val(v0[3]) + val(v1[3]));
        const float res = xres + rbf(rbf(z) * gate);
        __builtin_amdgcn_raw_buffer_store_b16((unsigned short)pk(res, res), ro, vout, sout, 0);
        SB();
        if (__all(tag_of(v0) == tag && tag_of(v1) == tag)) break;
      }
    }
  }
}
}

extern "C" void kernel_launch(void* const* d_in, const int* in_sizes, int n_in, void* d_out, int out_size, void* d_ws, size_t ws_size,
                              hipStream_t stream) {
  fused_decode<<<256, 512, 0, stream>>>((const u8*)d_in[0], (const u8*)d_in[1], (const u8*)d_in[2], (const u8*)d_in[3], (const u8*)d_in[4],
                                        (const u8*)d_in[6], (const u8*)d_in[7], (const u8*)d_in[8], (const u8*)d_in[9], (u8*)d_out, (u8*)d_ws);
}
```
